```python
import torch

def _softmax(x, axis=-1):
    return torch.softmax(x, dim=axis)
def _gelu(x):
    return torch.nn.functional.gelu(x, approximate="tanh")
def _silu(x):
    return torch.nn.functional.silu(x)
def _sigmoid(x):
    return torch.sigmoid(x)
def _layernorm(x, axis=-1, eps=1e-5):
    m = x.mean(dim=axis, keepdim=True)
    v = x.var(dim=axis, keepdim=True, unbiased=False)
    return (x - m) / torch.sqrt(v + eps)
def _scatter_add(tgt, idx, src, axis=0):
    return tgt.clone().index_add_(axis, idx, src)
def _segment_sum(src, seg, n):
    out = torch.zeros((n,) + src.shape[1:], dtype=src.dtype)
    return out.index_add_(0, seg, src)
def _pad(x, pw):
    flat = tuple(v for pair in reversed(pw) for v in pair)
    return torch.nn.functional.pad(x, flat)

kernel_name = 'standalone__paged_block_table_kv_pagedattention__decode'

def setup_inputs():

    _w = (lambda a: (lambda t: t.to(torch.bfloat16) if t.is_floating_point() else t)(torch.as_tensor(a)))
    B, Hq, Hkv, Sq, Skv, D = 2, 32, 16, 1, 8192, 128
    bs = 16
    nbps = max(1, Skv // bs)
    num_phys = B * nbps + 4
    bt = torch.randperm(num_phys)[: B * nbps].reshape(B, nbps).to(torch.int64)
    cl = (nbps * bs - (torch.arange(1, B + 1, dtype=torch.int64) * 2654435761) % max(1, nbps * bs // 2)).to(torch.int64)
    return {
        "q": _w((lambda *s: torch.randn(*s, dtype=torch.bfloat16) * 0.8)(B, Hq, Sq, D)),
        "k_cache": _w((lambda *s: torch.randn(*s, dtype=torch.bfloat16) * 0.8)(num_phys, bs, Hkv, D)),
        "v_cache": _w((lambda *s: torch.randn(*s, dtype=torch.bfloat16) * 0.05)(num_phys, bs, Hkv, D)),
        "block_table": _w(bt),
        "context_lens": _w(cl),
    }

def reference(q, k_cache, v_cache, block_table, context_lens):
    B, Hq, Sq, D = q.shape
    bs = k_cache.shape[1]
    Hkv = k_cache.shape[2]
    nbps = block_table.shape[1]
    Skv = nbps * bs
    bt = block_table.reshape(-1)
    k = (lambda x, *d: x.permute(*d))(k_cache[bt].reshape(B, Skv, Hkv, D), 0, 2, 1, 3)
    v = (lambda x, *d: x.permute(*d))(v_cache[bt].reshape(B, Skv, Hkv, D), 0, 2, 1, 3)
    g = Hq // Hkv
    qg = q.reshape(B, Hkv, g, Sq, D)
    scores = torch.einsum("bhgqd,bhkd->bhgqk", qg, k) * (D ** -0.5)
    pos = torch.arange(Skv)
    msk = pos[None, :] < context_lens[:, None]
    scores = torch.where(msk[:, None, None, None, :], scores, -1e9)
    p = _softmax(scores, axis=-1)
    out = torch.einsum("bhgqk,bhkd->bhgqd", p, v)
    return out.reshape(B, Hq, Sq, D)
```

```cpp
#include <hip/hip_runtime.h>
#include <hip/hip_bf16.h>
#include <stdint.h>

typedef __attribute__((ext_vector_type(8))) __bf16 bf16x8;
typedef __attribute__((ext_vector_type(4))) float f32x4;
typedef __attribute__((ext_vector_type(2))) float f32x2;
typedef __attribute__((ext_vector_type(4))) unsigned u32x4;

constexpr int NB = 2, HQ = 32, HKV = 16, D = 128, PAGE = 16, NBPS = 512, SKV = NBPS * PAGE;
constexpr int NSPLIT = 16;
constexpr int WPB = 4;
constexpr int PPW = NBPS / (NSPLIT * WPB);
constexpr int PAGE_BYTES = PAGE * HKV * D * 2;
constexpr int TOK_BYTES = HKV * D * 2;
#define KV_AUX 19

__device__ __forceinline__ float xg_max(float x) {
  auto s = __builtin_amdgcn_permlane16_swap(__float_as_uint(x), __float_as_uint(x), false, false);
  x = fmaxf(__uint_as_float(s[0]), __uint_as_float(s[1]));
  auto t = __builtin_amdgcn_permlane32_swap(__float_as_uint(x), __float_as_uint(x), false, false);
  return fmaxf(__uint_as_float(t[0]), __uint_as_float(t[1]));
}
__device__ __forceinline__ float xg_sum(float x) {
  auto s = __builtin_amdgcn_permlane16_swap(__float_as_uint(x), __float_as_uint(x), false, false);
  x = __uint_as_float(s[0]) + __uint_as_float(s[1]);
  auto t = __builtin_amdgcn_permlane32_swap(__float_as_uint(x), __float_as_uint(x), false, false);
  return __uint_as_float(t[0]) + __uint_as_float(t[1]);
}
__device__ __forceinline__ unsigned cvtpk(float lo, float hi) {
  typedef __attribute__((ext_vector_type(2))) __bf16 bf16x2;
  bf16x2 r = {(__bf16)lo, (__bf16)hi};
  return __builtin_bit_cast(unsigned, r);
}

__global__ void __launch_bounds__(512, 2)
attn_split(const uint16_t* __restrict__ q, const uint16_t* __restrict__ kc, const uint16_t* __restrict__ vc,
           const int* __restrict__ bt, const int* __restrict__ lens, float* __restrict__ pml, float* __restrict__ po,
           unsigned kv_bytes) {
  __shared__ __attribute__((aligned(16))) float lds[8 * 256 + 64];
  const int wg = blockIdx.x;
  const int kvh = wg & 15, s = wg >> 4;
  const int tid = threadIdx.x, lane = tid & 63;
  const int wave = __builtin_amdgcn_readfirstlane(tid >> 6);
  const int b = wave >> 2, wq = wave & 3;
  const int g = lane >> 4, c = lane & 15, hh = (lane >> 3) & 1;

  int len = lens[b];
  int phys[PPW];
  const int* btp = bt + b * NBPS + s + 16 * wq;
#pragma unroll
  for (int i = 0; i < PPW; ++i) phys[i] = btp[64 * i];
  bf16x8 qf[4];
  {
    const u32x4* qp = reinterpret_cast<const u32x4*>(q + (size_t)(b * HQ + kvh * 2 + hh) * D + 8 * g);
#pragma unroll
    for (int ks = 0; ks < 4; ++ks) qf[ks] = __builtin_bit_cast(bf16x8, qp[4 * ks]);
  }
  float c2 = 0.088388347648318440f * 1.4426950408889634f;
  if (len <= 0) { len = SKV; c2 = 0.f; }
  if (len > SKV) len = SKV;

  const unsigned koff = c * TOK_BYTES + g * 16 + kvh * 256;
  const unsigned voff = g * 4 * TOK_BYTES + c * 16 + kvh * 256;
  u32x4 kr[PPW][4], vr[PPW][4];
#pragma unroll
  for (int i = 0; i < PPW; ++i) {
    const int pg = s + 16 * (wq + 4 * i);
    const bool live = pg * PAGE < len;
    auto rs = __builtin_amdgcn_make_buffer_rsrc((void*)kc, 0, live ? kv_bytes : 0u, 0x00020000);
    const unsigned so = (unsigned)phys[i] * PAGE_BYTES;
#pragma unroll
    for (int ks = 0; ks < 4; ++ks) kr[i][ks] = __builtin_amdgcn_raw_buffer_load_b128(rs, koff + ks * 64, so, KV_AUX);
  }
  __builtin_amdgcn_sched_barrier(0);
  f32x4 sc[PPW];
#pragma unroll
  for (int i = 0; i < PPW; ++i) {
    f32x4 a = {0.f, 0.f, 0.f, 0.f};
#pragma unroll
    for (int ks = 0; ks < 4; ++ks) a = __builtin_amdgcn_mfma_f32_16x16x32_bf16(__builtin_bit_cast(bf16x8, kr[i][ks]), qf[ks], a, 0, 0, 0);
    sc[i] = a;
    __builtin_amdgcn_sched_barrier(0);
    const int pg = s + 16 * (wq + 4 * i);
    const bool live = pg * PAGE < len;
    auto rs = __builtin_amdgcn_make_buffer_rsrc((void*)vc, 0, live ? kv_bytes : 0u, 0x00020000);
    const unsigned so = (unsigned)phys[i] * PAGE_BYTES;
#pragma unroll
    for (int t = 0; t < 4; ++t) vr[i][t] = __builtin_amdgcn_raw_buffer_load_b128(rs, voff, so + t * TOK_BYTES, KV_AUX);
    __builtin_amdgcn_sched_barrier(0);
  }
  float mx = -INFINITY;
#pragma unroll
  for (int i = 0; i < PPW; ++i) {
    const int tok0 = (s + 16 * (wq + 4 * i)) * PAGE + 4 * g;
#pragma unroll
    for (int r = 0; r < 4; ++r) {
      const float v = (tok0 + r < len) ? sc[i][r] * c2 : -INFINITY;
      sc[i][r] = v;
      mx = fmaxf(mx, v);
    }
  }
  mx = xg_max(mx);
  float* mxs = lds + 8 * 256;
  if ((lane & 55) == 0) mxs[wave * 2 + hh] = mx;
  __syncthreads();
  {
    const float* p = mxs + b * 8 + hh;
    mx = fmaxf(fmaxf(p[0], p[2]), fmaxf(p[4], p[6]));
  }
  const float msub = (mx == -INFINITY) ? 0.f : mx;
  float lsum = 0.f;
  unsigned pk[PPW][4];
#pragma unroll
  for (int i = 0; i < PPW; ++i)
#pragma unroll
    for (int r = 0; r < 4; ++r) {
      const float p = __builtin_amdgcn_exp2f(sc[i][r] - msub);
      lsum += p;
      pk[i][r] = cvtpk(p, p);
    }
  lsum = xg_sum(lsum);
  unsigned msk[4];
#pragma unroll
  for (int k = 0; k < 4; ++k) msk[k] = (((lane & 7) >> 1) == k) ? ((lane & 1) ? 0xffff0000u : 0x0000ffffu) : 0u;
  f32x4 o = {0.f, 0.f, 0.f, 0.f};
  __builtin_amdgcn_sched_barrier(0);
#pragma unroll
  for (int i = 0; i < PPW; ++i) {
#pragma unroll
    for (int t = 0; t < 4; ++t) {
      u32x4 pb = {pk[i][t] & msk[0], pk[i][t] & msk[1], pk[i][t] & msk[2], pk[i][t] & msk[3]};
      o = __builtin_amdgcn_mfma_f32_16x16x32_bf16(__builtin_bit_cast(bf16x8, vr[i][t]), __builtin_bit_cast(bf16x8, pb), o, 0, 0, 0);
    }
  }
  float* ow = lds + wave * 256;
#pragma unroll
  for (int r = 0; r < 4; ++r) ow[hh * 128 + 8 * (4 * g + r) + (lane & 7)] = o[r];
  if ((lane & 55) == 0) mxs[16 + wave * 2 + hh] = lsum;
  __syncthreads();
  {
    const int b2 = tid >> 8, e = tid & 255;
    const float* src = lds + b2 * 1024 + e;
    const float sum = (src[0] + src[256]) + (src[512] + src[768]);
    const int row = b2 * HQ + kvh * 2 + (e >> 7);
    po[((size_t)row * NSPLIT + s) * D + (e & 127)] = sum;
    if ((e & 127) == 0) {
      const float* lp = mxs + 16 + b2 * 8 + (e >> 7);
      const float* mp = mxs + b2 * 8 + (e >> 7);
      const float L = (lp[0] + lp[2]) + (lp[4] + lp[6]);
      const float M = fmaxf(fmaxf(mp[0], mp[2]), fmaxf(mp[4], mp[6]));
      *reinterpret_cast<f32x2*>(pml + ((size_t)row * NSPLIT + s) * 2) = f32x2{M, L};
    }
  }
}

__global__ void __launch_bounds__(128) attn_combine(const float* __restrict__ pml, const float* __restrict__ po, uint16_t* __restrict__ out) {
  const int row = blockIdx.x, d = threadIdx.x;
  float m[NSPLIT], l[NSPLIT], o[NSPLIT];
#pragma unroll
  for (int s = 0; s < NSPLIT; ++s) {
    const f32x2 ml = *reinterpret_cast<const f32x2*>(pml + ((size_t)row * NSPLIT + s) * 2);
    m[s] = ml[0]; l[s] = ml[1];
    o[s] = po[((size_t)row * NSPLIT + s) * D + d];
  }
  float M = -INFINITY;
#pragma unroll
  for (int s = 0; s < NSPLIT; ++s) M = fmaxf(M, m[s]);
  float L = 0.f, acc = 0.f;
#pragma unroll
  for (int s = 0; s < NSPLIT; ++s) {
    const float w = __builtin_amdgcn_exp2f(m[s] - M);
    L = fmaf(w, l[s], L); acc = fmaf(w, o[s], acc);
  }
  const __bf16 r = (__bf16)(acc / L);
  out[row * D + d] = __builtin_bit_cast(uint16_t, r);
}

extern "C" void kernel_launch(void* const* d_in, const int* in_sizes, int n_in, void* d_out, int out_size, void* d_ws,
                              size_t ws_size, hipStream_t stream) {
  const uint16_t* q = (const uint16_t*)d_in[0];
  const uint16_t* k = (const uint16_t*)d_in[1];
  const uint16_t* v = (const uint16_t*)d_in[2];
  const int* bt = (const int*)d_in[3];
  const int* lens = (const int*)d_in[4];
  float* pml = (float*)d_ws;
  float* po = (float*)((char*)d_ws + 65536);
  const unsigned kv_bytes = (unsigned)in_sizes[1] * 2u;
  attn_split<<<HKV * NSPLIT, 512, 0, stream>>>(q, k, v, bt, lens, pml, po, kv_bytes);
  attn_combine<<<NB * HQ, 128, 0, stream>>>(pml, po, (uint16_t*)d_out);
}
```

```cpp
#include <hip/hip_runtime.h>
#include <hip/hip_bf16.h>
#include <stdint.h>

typedef __attribute__((ext_vector_type(8))) __bf16 bf16x8;
typedef __attribute__((ext_vector_type(4))) float f32x4;
typedef __attribute__((ext_vector_type(2))) float f32x2;
typedef __attribute__((ext_vector_type(4))) unsigned u32x4;

constexpr int NB = 2, HQ = 32, HKV = 16, D = 128, PAGE = 16, NBPS = 512, SKV = NBPS * PAGE;
constexpr int NSPLIT = 16;
constexpr int PPW = 8;
constexpr int PAGE_BYTES = PAGE * HKV * D * 2;
constexpr int TOK_BYTES = HKV * D * 2;
constexpr int WIN = 3;
constexpr int VORD[PPW] = {0, 7, 1, 6, 2, 5, 4, 3};
constexpr unsigned REC = 1152;
constexpr unsigned WS_REC = 4096, WS_BYTES = WS_REC + 32u * NSPLIT * REC;
constexpr unsigned OOB = 0xffffff00u;
constexpr int AUX_NT = 2, AUX_SC1 = 16;

template <int CTRL> __device__ __forceinline__ unsigned dppu(unsigned x) { return (unsigned)__builtin_amdgcn_mov_dpp((int)x, CTRL, 0xf, 0xf, true); }
template <int CTRL> __device__ __forceinline__ float dpp(float x) { return __uint_as_float(dppu<CTRL>(__float_as_uint(x))); }
constexpr int QP_X1 = 0xB1, QP_X2 = 0x4E, QP_0022 = 0xA0, HMIR = 0x141, ROR8 = 0x128;
__device__ __forceinline__ float xg_max(float x) {
  auto s = __builtin_amdgcn_permlane16_swap(__float_as_uint(x), __float_as_uint(x), false, false);
  x = fmaxf(__uint_as_float(s[0]), __uint_as_float(s[1]));
  auto t = __builtin_amdgcn_permlane32_swap(__float_as_uint(x), __float_as_uint(x), false, false);
  return fmaxf(__uint_as_float(t[0]), __uint_as_float(t[1]));
}
__device__ __forceinline__ float xg_sum(float x) {
  auto s = __builtin_amdgcn_permlane16_swap(__float_as_uint(x), __float_as_uint(x), false, false);
  x = __uint_as_float(s[0]) + __uint_as_float(s[1]);
  auto t = __builtin_amdgcn_permlane32_swap(__float_as_uint(x), __float_as_uint(x), false, false);
  return __uint_as_float(t[0]) + __uint_as_float(t[1]);
}
__device__ __forceinline__ unsigned line_sum(u32x4 w) {
  unsigned x = (w[0] + w[1]) + (w[2] + w[3]);
  x += dppu<QP_X1>(x); x += dppu<QP_X2>(x); x += dppu<HMIR>(x);
  return x;
}
__device__ __forceinline__ unsigned cvtpk(float lo, float hi) {
  typedef __attribute__((ext_vector_type(2))) __bf16 bf16x2;
  bf16x2 r = {(__bf16)lo, (__bf16)hi};
  return __builtin_bit_cast(unsigned, r);
}

__global__ void __launch_bounds__(512, 2)
attn_decode(const uint16_t* __restrict__ q, const uint16_t* __restrict__ kc, const uint16_t* __restrict__ vc,
            const int* __restrict__ bt, const int* __restrict__ lens, uint16_t* __restrict__ out, unsigned char* ws,
            unsigned kv_bytes) {
  __shared__ __attribute__((aligned(16))) unsigned char lds[8 * 1024 + 8 * 512];
  const int wg = blockIdx.x;
  const int lane = threadIdx.x & 63;
  const int wave = __builtin_amdgcn_readfirstlane(threadIdx.x >> 6);
  const int b = wave >> 2, sub = wave & 3;
  const int g = lane >> 4, c = lane & 15, hh = (lane >> 3) & 1;
  const int s = wg & 15, kvh = ((wg >> 4) + 2 * b) & 15;
  const int e = (s >> 1) | ((s & 1) << 3);
  const int pg0 = (b ? e : 15 - e) + NSPLIT * sub;
  const int grp = b * HKV + kvh;
  const unsigned rec = WS_REC + (unsigned)(grp * NSPLIT + s) * REC;

  typedef const __attribute__((address_space(4))) int* cint_p;
  typedef const __attribute__((address_space(4))) char* cchar_p;
  unsigned bt_off = (unsigned)(b * NBPS + pg0) * 4u, len_off = (unsigned)b * 4u;
  asm volatile("" : "+s"(bt_off), "+s"(len_off) : "s"(q), "s"(kc), "s"(vc), "s"(bt), "s"(lens), "s"(out), "s"(ws), "s"(kv_bytes));
  int phys[PPW];
#pragma unroll
  for (int i = 0; i < PPW; ++i) phys[i] = *(cint_p)((cchar_p)(uintptr_t)bt + bt_off + 256 * i);
  int len = *(cint_p)((cchar_p)(uintptr_t)lens + len_off);
  __builtin_amdgcn_sched_barrier(0);
  auto wrs = __builtin_amdgcn_make_buffer_rsrc((void*)ws, 0, WS_BYTES, 0x00020000);
  bf16x8 qf[2];
  {
    const u32x4* qp = reinterpret_cast<const u32x4*>(q + (size_t)(b * HQ + kvh * 2 + hh) * D + 8 * (2 * g + (lane & 1)));
    qf[0] = __builtin_bit_cast(bf16x8, qp[0]);
    qf[1] = __builtin_bit_cast(bf16x8, qp[8]);
  }
  const unsigned epoch = __hip_atomic_load(reinterpret_cast<unsigned*>(ws + grp * 128), __ATOMIC_RELAXED, __HIP_MEMORY_SCOPE_AGENT);
  const unsigned warm = __builtin_amdgcn_raw_buffer_load_b32(wrs, (sub == 1 && lane < 9) ? rec + lane * 128 : OOB, 0, 0);
  const unsigned koff = (lane & 7) * TOK_BYTES + (lane >> 3) * 16 + kvh * 256;
  const unsigned voff = g * 4 * TOK_BYTES + c * 16 + kvh * 256;
  const unsigned nothr = ((unsigned)(wg >> 4) + (unsigned)((uintptr_t)kc >> 8)) & 1u;
  __builtin_amdgcn_sched_barrier(0);

  float c2 = 0.088388347648318440f * 1.4426950408889634f;
  if (len <= 0) { len = SKV; c2 = 0.f; }
  if (len > SKV) len = SKV;
  u32x4 kv[2 * PPW][4];
  auto issue = [&](int n) {
    if (n >= WIN) asm volatile("s_cmp_lg_u32 %0, 0\n\ts_cbranch_scc1 .Lnothr%=\n\ts_waitcnt vmcnt(3)\n.Lnothr%=:" :: "s"(nothr) : "scc");
    const int i = n < PPW ? n : VORD[n - PPW];
    const bool live = (pg0 + 64 * i) * PAGE < len;
    auto rs = __builtin_amdgcn_make_buffer_rsrc((void*)(n < PPW ? kc : vc), 0, live ? kv_bytes : 0u, 0x00020000);
    const unsigned so = (unsigned)phys[i] * PAGE_BYTES;
#pragma unroll
    for (int t = 0; t < 4; ++t)
      kv[n][t] = n < PPW ? __builtin_amdgcn_raw_buffer_load_b128(rs, koff + (t & 1) * 128, so + (t >> 1) * 8 * TOK_BYTES, AUX_NT)
                         : __builtin_amdgcn_raw_buffer_load_b128(rs, voff, so + t * TOK_BYTES, AUX_NT);
  };
#pragma unroll
  for (int n = 0; n < WIN; ++n) issue(n);
  __builtin_amdgcn_sched_barrier(0);

  f32x4 sc[PPW];
  float mx = -INFINITY;
#pragma unroll
  for (int i = 0; i < PPW; ++i) {
    const f32x4 z = {0.f, 0.f, 0.f, 0.f};
    f32x4 a0 = __builtin_amdgcn_mfma_f32_16x16x32_bf16(__builtin_bit_cast(bf16x8, kv[i][0]), qf[0], z, 0, 0, 0);
    a0 = __builtin_amdgcn_mfma_f32_16x16x32_bf16(__builtin_bit_cast(bf16x8, kv[i][1]), qf[1], a0, 0, 0, 0);
    f32x4 a1 = __builtin_amdgcn_mfma_f32_16x16x32_bf16(__builtin_bit_cast(bf16x8, kv[i][2]), qf[0], z, 0, 0, 0);
    a1 = __builtin_amdgcn_mfma_f32_16x16x32_bf16(__builtin_bit_cast(bf16x8, kv[i][3]), qf[1], a1, 0, 0, 0);
    __builtin_amdgcn_sched_barrier(0);
    issue(i + WIN);
    __builtin_amdgcn_sched_barrier(0);
    const int tok0 = (pg0 + 64 * i) * PAGE + 4 * g;
#pragma unroll
    for (int r = 0; r < 4; ++r) {
      auto sw = __builtin_amdgcn_permlane32_swap(__float_as_uint(a0[r]), __float_as_uint(a1[r]), false, false);
      const float x = __uint_as_float(sw[0]) + dpp<QP_X1>(__uint_as_float(sw[1]));
      const float v = dpp<QP_0022>(x) * c2;
      sc[i][r] = (tok0 + r < len) ? v : -INFINITY;
    }
    mx = fmaxf(mx, fmaxf(fmaxf(sc[i][0], sc[i][1]), fmaxf(sc[i][2], sc[i][3])));
    __builtin_amdgcn_sched_barrier(0);
  }
  mx = xg_max(mx);
  const float msub = (mx == -INFINITY) ? 0.f : mx;
  float lsum = 0.f;
  unsigned msk[4];
#pragma unroll
  for (int k = 0; k < 4; ++k) msk[k] = (((lane & 7) >> 1) == k) ? ((lane & 1) ? 0xffff0000u : 0x0000ffffu) : 0u;
  unsigned tag = epoch * 0x9E3779B1u + 0x7F4A7C15u;
  const int line = s & 7;
  unsigned a_sd = rec + lane * 16, a_sh = (lane & 7) == 0 ? rec + 1024 + lane * 2 : OOB;
  unsigned a_ld = WS_REC + (unsigned)(grp * NSPLIT + (lane >> 3)) * REC + line * 128 + (lane & 7) * 16;
  unsigned a_lh = WS_REC + (unsigned)(grp * NSPLIT + (lane >> 3)) * REC + 1024 + line * 16;
  unsigned a_out = hh == 0 ? 2u * ((b * HQ + kvh * 2 + (line & 1)) * D + 32 * (line >> 1) + 8 * g + (lane & 7)) : OOB;
  unsigned a_ep = (line == 0 && lane == 0) ? (unsigned)grp * 128u : OOB;
  unsigned l_o = wave * 1024 + lane * 16, l_ml = 8192 + wave * 512 + lane * 8;
  asm volatile("" : "+v"(tag), "+v"(a_sd), "+v"(a_sh), "+v"(a_ld), "+v"(a_lh), "+v"(a_out), "+v"(a_ep), "+v"(l_o), "+v"(l_ml) : "v"(warm));

  f32x4 o = {0.f, 0.f, 0.f, 0.f};
#pragma unroll
  for (int i = 0; i < PPW; ++i) {
    u32x4 pb[4];
#pragma unroll
    for (int r = 0; r < 4; ++r) {
      const float p = __builtin_amdgcn_exp2f(sc[VORD[i]][r] - msub);
      lsum += p;
      const unsigned pk = cvtpk(p, p);
      pb[r] = u32x4{pk & msk[0], pk & msk[1], pk & msk[2], pk & msk[3]};
    }
    if (i == PPW - 1) *reinterpret_cast<f32x2*>(lds + l_ml) = f32x2{mx, xg_sum(lsum)};
    __builtin_amdgcn_sched_barrier(0);
#pragma unroll
    for (int t = 0; t < 4; ++t)
      o = __builtin_amdgcn_mfma_f32_16x16x32_bf16(__builtin_bit_cast(bf16x8, kv[PPW + i][t]), __builtin_bit_cast(bf16x8, pb[t]), o, 0, 0, 0);
    __builtin_amdgcn_sched_barrier(0);
    if (PPW + i + WIN < 2 * PPW) issue(PPW + i + WIN);
    __builtin_amdgcn_sched_barrier(0);
  }
  *reinterpret_cast<f32x4*>(lds + l_o) = o;
  asm volatile("s_waitcnt lgkmcnt(0)\n\ts_barrier" ::: "memory");

  if (sub == 0) {
    f32x2 ml[4]; f32x4 ov[4];
#pragma unroll
    for (int w = 0; w < 4; ++w) {
      ml[w] = *reinterpret_cast<const f32x2*>(lds + l_ml + w * 512);
      ov[w] = *reinterpret_cast<const f32x4*>(lds + l_o + w * 1024);
    }
    const float m2 = fmaxf(fmaxf(ml[0][0], ml[1][0]), fmaxf(ml[2][0], ml[3][0]));
    const float ms2 = (m2 == -INFINITY) ? 0.f : m2;
    float l2 = 0.f; f32x4 o2 = {0.f, 0.f, 0.f, 0.f};
#pragma unroll
    for (int w = 0; w < 4; ++w) {
      const float wt = __builtin_amdgcn_exp2f(ml[w][0] - ms2);
      l2 = fmaf(wt, ml[w][1], l2);
      o2 += wt * ov[w];
    }
    const u32x4 ow = __builtin_bit_cast(u32x4, o2);
    __builtin_amdgcn_raw_buffer_store_b128(ow, wrs, a_sd, 0, AUX_SC1);
    const unsigned ck = tag ^ line_sum(ow);
    __builtin_amdgcn_raw_buffer_store_b128(u32x4{__float_as_uint(m2), ck, __float_as_uint(l2), ck}, wrs, a_sh, 0, AUX_SC1);
  } else if (sub == 1 && b == (s >> 3)) {
    float x0, x1, x2, x3, y0, y1, y2, y3, pm0, pl0, pm1, pl1;
    unsigned t0, t1, t2, t3, cnt;
    unsigned long long p0, p1, pt;
    u32x4 k0, k1, k2, k3, k4, k5, k6, k7, k8, k9, k10, k11;
    const u32x4 srd = {(unsigned)(uintptr_t)ws, (unsigned)((uintptr_t)ws >> 32) & 0xffffu, WS_BYTES, 0x00020000u};
    const unsigned a_ld1 = a_ld + 8 * REC, a_lh1 = a_lh + 8 * REC, oob = OOB;
#define PLOAD(R) \
    "v_cndmask_b32 %[t0], %[oob], %[ad0], %[p0]\n\t" "v_cndmask_b32 %[t1], %[oob], %[ah0], %[p0]\n\t" \
    "v_cndmask_b32 %[t2], %[oob], %[ad1], %[p1]\n\t" "v_cndmask_b32 %[t3], %[oob], %[ah1], %[p1]\n\t" \
    "buffer_load_dwordx4 v[" #R ":" #R "+3], %[t0], %[srd], 0 offen sc1\n\t" \
    "buffer_load_dwordx4 v[" #R "+4:" #R "+7], %[t1], %[srd], 0 offen sc1\n\t" \
    "buffer_load_dwordx4 v[" #R "+8:" #R "+11], %[t2], %[srd], 0 offen sc1\n\t" \
    "buffer_load_dwordx4 v[" #R "+12:" #R "+15], %[t3], %[srd], 0 offen sc1\n\t"
#define PDPP(CTRL) "s_nop 0\n\t" \
    "v_add_u32_dpp %[t0], %[t0], %[t0] " CTRL " row_mask:0xf bank_mask:0xf\n\t" "v_add_u32_dpp %[t1], %[t1], %[t1] " CTRL " row_mask:0xf bank_mask:0xf\n\t"
#define PCHECK(R) \
    "v_add3_u32 %[t0], v[" #R "], v[" #R "+1], v[" #R "+2]\n\t" "v_add3_u32 %[t1], v[" #R "+8], v[" #R "+9], v[" #R "+10]\n\t" \
    "v_add_u32 %[t0], %[t0], v[" #R "+3]\n\t" "v_add_u32 %[t1], %[t1], v[" #R "+11]\n\t" \
    PDPP("quad_perm:[1,0,3,2]") PDPP("quad_perm:[2,3,0,1]") PDPP("row_half_mirror") \
    "v_xor_b32 %[t0], %[t0], %[tag]\n\t" "v_xor_b32 %[t1], %[t1], %[tag]\n\t" \
    "s_mov_b64 exec, %[p0]\n\t" \
    "v_cmpx_eq_u32_e32 vcc, %[t0], v[" #R "+5]\n\t" "v_cmpx_eq_u32_e32 vcc, %[t0], v[" #R "+7]\n\t" \
    "v_mov_b32 %[x0], v[" #R "]\n\t" "v_mov_b32 %[x1], v[" #R "+1]\n\t" "v_mov_b32 %[x2], v[" #R "+2]\n\t" "v_mov_b32 %[x3], v[" #R "+3]\n\t" \
    "v_mov_b32 %[pm0], v[" #R "+4]\n\t" "v_mov_b32 %[pl0], v[" #R "+6]\n\t" \
    "s_andn2_b64 %[p0], %[p0], exec\n\t" \
    "s_mov_b64 exec, %[p1]\n\t" \
    "v_cmpx_eq_u32_e32 vcc, %[t1], v[" #R "+13]\n\t" "v_cmpx_eq_u32_e32 vcc, %[t1], v[" #R "+15]\n\t" \
    "v_mov_b32 %[y0], v[" #R "+8]\n\t" "v_mov_b32 %[y1], v[" #R "+9]\n\t" "v_mov_b32 %[y2], v[" #R "+10]\n\t" "v_mov_b32 %[y3], v[" #R "+11]\n\t" \
    "v_mov_b32 %[pm1], v[" #R "+12]\n\t" "v_mov_b32 %[pl1], v[" #R "+14]\n\t" \
    "s_andn2_b64 %[p1], %[p1], exec\n\t" \
    "s_mov_b64 exec, -1\n\t" \
    "s_or_b64 %[pt], %[p0], %[p1]\n\t" "s_cbranch_scc0 .Lpdone%=\n\t"
    asm volatile(
      "s_mov_b64 %[p0], -1\n\t"
      "s_mov_b64 %[p1], -1\n\t"
      PLOAD(112)
      "s_waitcnt vmcnt(0)\n\t"
      PCHECK(112)
      PLOAD(112) "s_sleep 5\n\t"
      PLOAD(128) "s_sleep 5\n\t"
      PLOAD(144)
      "s_mov_b32 %[cnt], 0x40000\n"
      ".Lploop%=:\n\t"
      "s_waitcnt vmcnt(8)\n\t" PCHECK(112) PLOAD(112)
      "s_waitcnt vmcnt(8)\n\t" PCHECK(128) PLOAD(128)
      "s_waitcnt vmcnt(8)\n\t" PCHECK(144) PLOAD(144)
      "s_sub_u32 %[cnt], %[cnt], 1\n\t"
      "s_cmp_lg_u32 %[cnt], 0\n\t"
      "s_cbranch_scc1 .Lploop%=\n"
      ".Lpdone%=:\n\t"
      "s_nop 1"
      : [x0] "=&v"(x0), [x1] "=&v"(x1), [x2] "=&v"(x2), [x3] "=&v"(x3), [y0] "=&v"(y0), [y1] "=&v"(y1), [y2] "=&v"(y2), [y3] "=&v"(y3),
        [pm0] "=&v"(pm0), [pl0] "=&v"(pl0), [pm1] "=&v"(pm1), [pl1] "=&v"(pl1), [t0] "=&v"(t0), [t1] "=&v"(t1), [t2] "=&v"(t2), [t3] "=&v"(t3),
        [p0] "=&s"(p0), [p1] "=&s"(p1), [pt] "=&s"(pt), [cnt] "=&s"(cnt),
        "=&{v[112:115]}"(k0), "=&{v[116:119]}"(k1), "=&{v[120:123]}"(k2), "=&{v[124:127]}"(k3), "=&{v[128:131]}"(k4), "=&{v[132:135]}"(k5),
        "=&{v[136:139]}"(k6), "=&{v[140:143]}"(k7), "=&{v[144:147]}"(k8), "=&{v[148:151]}"(k9), "=&{v[152:155]}"(k10), "=&{v[156:159]}"(k11)
      : [ad0] "v"(a_ld), [ah0] "v"(a_lh), [ad1] "v"(a_ld1), [ah1] "v"(a_lh1), [oob] "v"(oob), [tag] "v"(tag), [srd] "s"(srd)
      : "vcc", "scc", "memory");
    if (pt == 0ull) {
      float M = fmaxf(pm0, pm1);
      M = xg_max(fmaxf(M, dpp<ROR8>(M)));
      const float w0 = __builtin_amdgcn_exp2f(pm0 - M), w1 = __builtin_amdgcn_exp2f(pm1 - M);
      float L = fmaf(w1, pl1, w0 * pl0);
      L = xg_sum(L + dpp<ROR8>(L));
      const float r0 = fmaf(w1, y0, w0 * x0), r1 = fmaf(w1, y1, w0 * x1), r2 = fmaf(w1, y2, w0 * x2), r3 = fmaf(w1, y3, w0 * x3);
      auto s02 = __builtin_amdgcn_permlane32_swap(__float_as_uint(r0), __float_as_uint(r2), false, false);
      auto s13 = __builtin_amdgcn_permlane32_swap(__float_as_uint(r1), __float_as_uint(r3), false, false);
      auto s4 = __builtin_amdgcn_permlane16_swap(__float_as_uint(__uint_as_float(s02[0]) + __uint_as_float(s02[1])),
                                                 __float_as_uint(__uint_as_float(s13[0]) + __uint_as_float(s13[1])), false, false);
      float rv = __uint_as_float(s4[0]) + __uint_as_float(s4[1]);
      rv += dpp<ROR8>(rv);
      const __bf16 res = (__bf16)(rv * __builtin_amdgcn_rcpf(L));
      auto ors = __builtin_amdgcn_make_buffer_rsrc((void*)out, 0, NB * HQ * D * 2, 0x00020000);
      __builtin_amdgcn_raw_buffer_store_b16(__builtin_bit_cast(uint16_t, res), ors, a_out, 0, 0);
      __builtin_amdgcn_raw_buffer_store_b32(tag, wrs, a_ep, 0, AUX_SC1);
    }
    asm volatile("s_waitcnt vmcnt(0)" :: "{v[112:115]}"(k0), "{v[116:119]}"(k1), "{v[120:123]}"(k2), "{v[124:127]}"(k3), "{v[128:131]}"(k4),
                 "{v[132:135]}"(k5), "{v[136:139]}"(k6), "{v[140:143]}"(k7), "{v[144:147]}"(k8), "{v[148:151]}"(k9), "{v[152:155]}"(k10),
                 "{v[156:159]}"(k11) : "memory");
  }
}

extern "C" void kernel_launch(void* const* d_in, const int* in_sizes, int n_in, void* d_out, int out_size, void* d_ws,
                              size_t ws_size, hipStream_t stream) {
  const unsigned kv_bytes = (unsigned)in_sizes[1] * 2u;
  attn_decode<<<HKV * NSPLIT, 512, 0, stream>>>((const uint16_t*)d_in[0], (const uint16_t*)d_in[1], (const uint16_t*)d_in[2],
                                                (const int*)d_in[3], (const int*)d_in[4], (uint16_t*)d_out, (unsigned char*)d_ws, kv_bytes);
}
```
